```python
import torch

def _softmax(x, axis=-1):
    return torch.softmax(x, dim=axis)
def _gelu(x):
    return torch.nn.functional.gelu(x, approximate="tanh")
def _silu(x):
    return torch.nn.functional.silu(x)
def _sigmoid(x):
    return torch.sigmoid(x)
def _layernorm(x, axis=-1, eps=1e-5):
    m = x.mean(dim=axis, keepdim=True)
    v = x.var(dim=axis, keepdim=True, unbiased=False)
    return (x - m) / torch.sqrt(v + eps)
def _scatter_add(tgt, idx, src, axis=0):
    return tgt.clone().index_add_(axis, idx, src)
def _segment_sum(src, seg, n):
    out = torch.zeros((n,) + src.shape[1:], dtype=src.dtype)
    return out.index_add_(0, seg, src)
def _pad(x, pw):
    flat = tuple(v for pair in reversed(pw) for v in pair)
    return torch.nn.functional.pad(x, flat)

kernel_name = 'parallel_sum__mha_block_cached__swiglu_ffn__decode'

def setup_inputs():

    _w = (lambda a: (lambda t: t.to(torch.bfloat16) if t.is_floating_point() else t)(torch.as_tensor(a)))
    def _A_setup():
        B, Sq, D = 1, 1, 640
        H, Sk, Dh = 32, 16384, 128
        return {
            "x": _w((lambda *s: torch.randn(*s, dtype=torch.bfloat16) * 0.05)(B, Sq, D)),
            "k_cache": _w((lambda *s: torch.randn(*s, dtype=torch.bfloat16) * 3.2)(B, H, Sk, Dh)),
            "v_cache": _w((lambda *s: torch.randn(*s, dtype=torch.bfloat16) * 1.6)(B, H, Sk, Dh)),
            "wq": _w((lambda *s: torch.randn(*s, dtype=torch.bfloat16) * 3.2)(D, H * Dh) * (D ** -0.5)),
            "wo": _w((lambda *s: torch.randn(*s, dtype=torch.bfloat16) * 1.6)(H * Dh, D) * ((H * Dh) ** -0.5)),
        }
    _A_in = _A_setup()

    def _B_setup():
        B, S, D, F = 1, 1, 640, 1728
        return {
            "x": _w((lambda *s: torch.randn(*s, dtype=torch.bfloat16) * 0.05)(B, S, D)),
            "w_gate": _w((lambda *s: torch.randn(*s, dtype=torch.bfloat16) * 0.05)(D, F)),
            "w_up": _w((lambda *s: torch.randn(*s, dtype=torch.bfloat16) * 0.05)(D, F)),
            "w_down": _w((lambda *s: torch.randn(*s, dtype=torch.bfloat16) * 0.05)(F, D)),
        }
    _B_in = _B_setup()

    return {
        "A_x": _A_in["x"],
        "A_k_cache": _A_in["k_cache"],
        "A_v_cache": _A_in["v_cache"],
        "A_wq": _A_in["wq"],
        "A_wo": _A_in["wo"],
        "B_x": _B_in["x"],
        "B_w_gate": _B_in["w_gate"],
        "B_w_up": _B_in["w_up"],
        "B_w_down": _B_in["w_down"],
    }

def reference(A_x, A_k_cache, A_v_cache, A_wq, A_wo, B_x, B_w_gate, B_w_up, B_w_down):
    def _A_ref(x, k_cache, v_cache, wq, wo):
        B, Sq, D = x.shape
        H, Dh = 32, 128
        q = (lambda x, *d: x.permute(*d))((x @ wq).reshape(B, Sq, H, Dh), 0, 2, 1, 3)
        scores = torch.einsum("bhqd,bhkd->bhqk", q, k_cache) * (Dh ** -0.5)
        p = _softmax(scores, axis=-1)
        o = torch.einsum("bhqk,bhkd->bhqd", p, v_cache)
        return (lambda x, *d: x.permute(*d))(o, 0, 2, 1, 3).reshape(B, Sq, H * Dh) @ wo

    def _B_ref(x, w_gate, w_up, w_down):
        g = _silu(x @ w_gate)
        u = x @ w_up
        return (g * u) @ w_down

    x = A_x
    xn = _layernorm(x, axis=-1)
    return x + _A_ref(xn, A_k_cache, A_v_cache, A_wq, A_wo) + _B_ref(xn, B_w_gate, B_w_up, B_w_down)
```

```cpp
#include <hip/hip_runtime.h>
#include <hip/hip_bf16.h>
#include <stdint.h>

typedef unsigned short u16;
typedef unsigned v4u __attribute__((ext_vector_type(4)));
typedef __bf16 bf16x2 __attribute__((ext_vector_type(2)));

constexpr int D = 640, H = 32, SK = 16384, DH = 128, F = 1728;
constexpr int NSPLIT = 16;

__device__ __forceinline__ float bf2f(u16 x) { return __uint_as_float(((unsigned)x) << 16); }
__device__ __forceinline__ float rbf(float x) { return (float)(__bf16)x; }
__device__ __forceinline__ u16 f2bf(float x) { __bf16 b = (__bf16)x; return __builtin_bit_cast(u16, b); }
__device__ __forceinline__ float dot2(unsigned k, unsigned q, float acc) {
  return __builtin_amdgcn_fdot2_f32_bf16(__builtin_bit_cast(bf16x2, k), __builtin_bit_cast(bf16x2, q), acc, false);
}

__device__ void layernorm_to_lds(const u16* x, float* xn, float* red) {
  const int tid = threadIdx.x;
  float s = 0.f;
  for (int i = tid; i < D; i += blockDim.x) s += bf2f(x[i]);
  for (int o = 32; o; o >>= 1) s += __shfl_xor(s, o);
  if ((tid & 63) == 0) red[tid >> 6] = s;
  __syncthreads();
  float tot = 0.f;
  for (int w = 0; w < (int)(blockDim.x >> 6); ++w) tot += red[w];
  const float mean = tot / D;
  __syncthreads();
  float v = 0.f;
  for (int i = tid; i < D; i += blockDim.x) { float d = bf2f(x[i]) - mean; v += d * d; }
  for (int o = 32; o; o >>= 1) v += __shfl_xor(v, o);
  if ((tid & 63) == 0) red[tid >> 6] = v;
  __syncthreads();
  float vt = 0.f;
  for (int w = 0; w < (int)(blockDim.x >> 6); ++w) vt += red[w];
  const float mb = rbf(mean), vb = rbf(vt / D);
  const float den = rbf(sqrtf(rbf(vb + 1e-5f)));
  for (int i = tid; i < D; i += blockDim.x) xn[i] = rbf(rbf(bf2f(x[i]) - mb) / den);
  __syncthreads();
}

__global__ void __launch_bounds__(256) k_prep(const u16* x, const u16* wq, const u16* wg, const u16* wu, u16* q, float* hid) {
  __shared__ float xn[D];
  __shared__ float red[8];
  __shared__ float part[2][4][64];
  layernorm_to_lds(x, xn, red);
  const int lane = threadIdx.x & 63, w = threadIdx.x >> 6;
  if (blockIdx.x < 64) {
    const int col = blockIdx.x * 64 + lane;
    float a = 0.f;
    for (int r = w * 160; r < w * 160 + 160; ++r) a += xn[r] * bf2f(wq[(size_t)r * 4096 + col]);
    part[0][w][lane] = a;
    __syncthreads();
    if (w == 0) q[col] = f2bf(part[0][0][lane] + part[0][1][lane] + part[0][2][lane] + part[0][3][lane]);
  } else {
    const int col = (blockIdx.x - 64) * 64 + lane;
    float a = 0.f, b = 0.f;
    for (int r = w * 160; r < w * 160 + 160; ++r) { a += xn[r] * bf2f(wg[(size_t)r * F + col]); b += xn[r] * bf2f(wu[(size_t)r * F + col]); }
    part[0][w][lane] = a; part[1][w][lane] = b;
    __syncthreads();
    if (w == 0) {
      float g = rbf(part[0][0][lane] + part[0][1][lane] + part[0][2][lane] + part[0][3][lane]);
      float u = rbf(part[1][0][lane] + part[1][1][lane] + part[1][2][lane] + part[1][3][lane]);
      float sg = rbf(g / (1.f + __expf(-g)));
      hid[col] = rbf(sg * u);
    }
  }
}

template <int CTRL> __device__ __forceinline__ float dpp(float x) {
  return __builtin_bit_cast(float, __builtin_amdgcn_mov_dpp(__builtin_bit_cast(int, x), CTRL, 0xf, 0xf, true));
}

__global__ void __launch_bounds__(256) k_attn(const u16* q, const u16* K, const u16* V, float* part) {
  __shared__ float mrg[4][4][132];
  const int h = blockIdx.x / NSPLIT, s = blockIdx.x % NSPLIT;
  const int lane = threadIdx.x & 63, w = threadIdx.x >> 6, r = lane >> 4, c = lane & 15;
  const v4u qv = *(const v4u*)(q + h * DH + 8 * c);
  constexpr int KPS = SK / NSPLIT;
  const u16* kb = K + ((size_t)h * SK + s * KPS) * DH;
  const u16* vb = V + ((size_t)h * SK + s * KPS) * DH;
  float m = -INFINITY, l = 0.f, acc[8];
  for (int i = 0; i < 8; ++i) acc[i] = 0.f;
  for (int t = w * 16; t < KPS; t += 64) {
    v4u kk[4], vv[4];
#pragma unroll
    for (int i = 0; i < 4; ++i) {
      kk[i] = __builtin_nontemporal_load((const v4u*)(kb + (size_t)(t + 4 * i) * DH + lane * 8));
      vv[i] = __builtin_nontemporal_load((const v4u*)(vb + (size_t)(t + 4 * i) * DH + lane * 8));
    }
    float sc[4];
#pragma unroll
    for (int i = 0; i < 4; ++i) {
      float d = 0.f;
      for (int k = 0; k < 4; ++k) d = dot2(kk[i][k], qv[k], d);
      d += dpp<0xB1>(d); d += dpp<0x4E>(d); d += dpp<0x141>(d); d += dpp<0x128>(d);
      sc[i] = rbf(rbf(d) * 0.088388347648318440f) * 1.4426950408889634f;
    }
    float mn = fmaxf(fmaxf(fmaxf(sc[0], sc[1]), fmaxf(sc[2], sc[3])), m);
    const float al = __builtin_amdgcn_exp2f(m - mn);
    m = mn; l *= al;
    for (int k = 0; k < 8; ++k) acc[k] *= al;
#pragma unroll
    for (int i = 0; i < 4; ++i) {
      const float p = __builtin_amdgcn_exp2f(sc[i] - mn);
      l += p;
      for (int k = 0; k < 4; ++k) {
        acc[2 * k] += p * __uint_as_float(vv[i][k] << 16);
        acc[2 * k + 1] += p * __uint_as_float(vv[i][k] & 0xffff0000u);
      }
    }
  }
  for (int k = 0; k < 8; ++k) mrg[w][r][4 + 8 * c + k] = acc[k];
  if (c == 0) { mrg[w][r][0] = m; mrg[w][r][1] = l; }
  __syncthreads();
  if (threadIdx.x < 128) {
    float M = -INFINITY;
    for (int j = 0; j < 16; ++j) M = fmaxf(M, mrg[j >> 2][j & 3][0]);
    float L = 0.f, o = 0.f;
    for (int j = 0; j < 16; ++j) { float wt = __builtin_amdgcn_exp2f(mrg[j >> 2][j & 3][0] - M); L += wt * mrg[j >> 2][j & 3][1]; o += wt * mrg[j >> 2][j & 3][4 + threadIdx.x]; }
    float* pp = part + (size_t)blockIdx.x * 132;
    pp[4 + threadIdx.x] = o;
    if (threadIdx.x == 0) { pp[0] = M; pp[1] = L; }
  }
}

__global__ void __launch_bounds__(1024) k_out(const u16* x, const float* part, const float* hid, const u16* wo, const u16* wd, u16* out) {
  __shared__ float o[4096];
  __shared__ float red[2][16][64];
  for (int i = threadIdx.x; i < 4096; i += 1024) {
    const int h = i >> 7, d = i & 127;
    float M = -INFINITY;
    for (int s = 0; s < NSPLIT; ++s) M = fmaxf(M, part[(size_t)(h * NSPLIT + s) * 132]);
    float L = 0.f, a = 0.f;
    for (int s = 0; s < NSPLIT; ++s) { const float* pp = part + (size_t)(h * NSPLIT + s) * 132; float wt = __builtin_amdgcn_exp2f(pp[0] - M); L += wt * pp[1]; a += wt * pp[4 + d]; }
    o[i] = rbf(a / L);
  }
  __syncthreads();
  const int lane = threadIdx.x & 63, w = threadIdx.x >> 6, col = blockIdx.x * 64 + lane;
  float a = 0.f, b = 0.f;
  for (int r = w * 256; r < w * 256 + 256; ++r) a += o[r] * bf2f(wo[(size_t)r * D + col]);
  for (int r = w * 108; r < w * 108 + 108; ++r) b += hid[r] * bf2f(wd[(size_t)r * D + col]);
  red[0][w][lane] = a; red[1][w][lane] = b;
  __syncthreads();
  if (w == 0) {
    float A = 0.f, B = 0.f;
    for (int j = 0; j < 16; ++j) { A += red[0][j][lane]; B += red[1][j][lane]; }
    out[col] = f2bf(rbf(bf2f(x[col]) + rbf(A)) + rbf(B));
  }
}

extern "C" void kernel_launch(void* const* d_in, const int* in_sizes, int n_in, void* d_out, int out_size, void* d_ws, size_t ws_size, hipStream_t stream) {
  const u16* x = (const u16*)d_in[0]; const u16* K = (const u16*)d_in[1]; const u16* V = (const u16*)d_in[2];
  const u16* wq = (const u16*)d_in[3]; const u16* wo = (const u16*)d_in[4];
  const u16* wg = (const u16*)d_in[6]; const u16* wu = (const u16*)d_in[7]; const u16* wd = (const u16*)d_in[8];
  char* ws = (char*)d_ws;
  u16* q = (u16*)ws; float* hid = (float*)(ws + 16384); float* part = (float*)(ws + 32768);
  k_prep<<<91, 256, 0, stream>>>(x, wq, wg, wu, q, hid);
  k_attn<<<H * NSPLIT, 256, 0, stream>>>(q, K, V, part);
  k_out<<<10, 1024, 0, stream>>>(x, part, hid, wo, wd, (u16*)d_out);
}
```

```cpp
#include <hip/hip_runtime.h>
#include <stdint.h>

#define INFL 20
#define KV_AUX 2
#define W1_AUX 2
#define W2_AUX 2

namespace {

typedef unsigned short u16;
typedef unsigned v4u __attribute__((ext_vector_type(4)));
typedef unsigned v2u __attribute__((ext_vector_type(2)));
typedef float v4f __attribute__((ext_vector_type(4)));
typedef float v2f __attribute__((ext_vector_type(2)));
typedef __bf16 bf16x8 __attribute__((ext_vector_type(8)));
#define LAS __attribute__((address_space(3)))

constexpr int D = 640, H = 32, SK = 16384, DH = 128, F = 1728;
constexpr int NS = 32;
constexpr int SLOT_K = 10, SLOT_V = SLOT_K + 128, SLOT_W2 = SLOT_V + 128;
constexpr int NFFN = F / 64;

constexpr int L_RING = 0;
constexpr int L_SC = NS * 4096;
constexpr int L_RED = L_SC + 8192;
constexpr int L_Q = L_RED + 4096;
constexpr int L_GU = L_Q + 256;
constexpr int L_ML = L_GU + 512;
constexpr int L_DONE = L_ML + 32;
constexpr int L_FULL = L_DONE + 16;
constexpr int L_CNT = L_FULL + 16;
constexpr int L_TAG = L_CNT + 16;
constexpr int LDS_BYTES = L_TAG + 16;

constexpr int WS_EPOCH = 0;
constexpr int WS_X1 = 4096;
constexpr int WS_X2 = WS_X1 + 256 * 2048;
constexpr int X2_STRIDE = 1152;
constexpr int WS_X3 = WS_X2 + 256 * X2_STRIDE;
constexpr int WS_END = WS_X3 + 256 * 640;
constexpr int SPIN_CAP = 1 << 20;

__device__ __forceinline__ float bf2f(unsigned x) { return __uint_as_float(x << 16); }
__device__ __forceinline__ unsigned f2bf(float x) { return __builtin_bit_cast(u16, (__bf16)x); }
__device__ __forceinline__ float rbf(float x) { return (float)(__bf16)x; }

template <int CTRL> __device__ __forceinline__ float dpp(float x) {
  return __builtin_bit_cast(float, __builtin_amdgcn_mov_dpp(__builtin_bit_cast(int, x), CTRL, 0xf, 0xf, true));
}
constexpr int XOR1 = 0xB1, XOR2 = 0x4E, HMIR = 0x141, ROR4 = 0x124, ROR8 = 0x128;
__device__ __forceinline__ float rows_sum(float x) {
  auto s = __builtin_amdgcn_permlane16_swap(__float_as_uint(x), __float_as_uint(x), false, false);
  x = __uint_as_float(s[0]) + __uint_as_float(s[1]);
  auto t = __builtin_amdgcn_permlane32_swap(__float_as_uint(x), __float_as_uint(x), false, false);
  return __uint_as_float(t[0]) + __uint_as_float(t[1]);
}
__device__ __forceinline__ float rows_max(float x) {
  auto s = __builtin_amdgcn_permlane16_swap(__float_as_uint(x), __float_as_uint(x), false, false);
  x = fmaxf(__uint_as_float(s[0]), __uint_as_float(s[1]));
  auto t = __builtin_amdgcn_permlane32_swap(__float_as_uint(x), __float_as_uint(x), false, false);
  return fmaxf(__uint_as_float(t[0]), __uint_as_float(t[1]));
}
__device__ __forceinline__ float halves_sum(float x) {
  auto t = __builtin_amdgcn_permlane32_swap(__float_as_uint(x), __float_as_uint(x), false, false);
  return __uint_as_float(t[0]) + __uint_as_float(t[1]);
}
__device__ __forceinline__ float wave_sum(float x) {
  x += dpp<XOR1>(x); x += dpp<XOR2>(x); x += dpp<HMIR>(x); x += dpp<ROR8>(x);
  return rows_sum(x);
}
__device__ __forceinline__ float wave_max(float x) {
  x = fmaxf(x, dpp<XOR1>(x)); x = fmaxf(x, dpp<XOR2>(x)); x = fmaxf(x, dpp<HMIR>(x)); x = fmaxf(x, dpp<ROR8>(x));
  return rows_max(x);
}

template <typename T> __device__ __forceinline__ T lds_ld(int off) { return *(const LAS T*)(size_t)off; }
template <typename T> __device__ __forceinline__ void lds_st(int off, T v) { *(LAS T*)(size_t)off = v; }

__device__ __forceinline__ unsigned ctl_read(int off) {
  unsigned r;
  asm volatile("ds_read_b32 %0, %1\n\ts_waitcnt lgkmcnt(0)" : "=v"(r) : "v"(off) : "memory");
  return __builtin_amdgcn_readfirstlane(r);
}
__device__ __forceinline__ void ctl_write(int off, unsigned v) {
  asm volatile("s_waitcnt lgkmcnt(0)\n\tds_write_b32 %0, %1" :: "v"(off), "v"(v) : "memory");
}
__device__ __forceinline__ unsigned ctl_arrive(int off, int lane) {
  unsigned r = 0;
  asm volatile("s_waitcnt lgkmcnt(0)" ::: "memory");
  if (lane == 0) asm volatile("ds_add_rtn_u32 %0, %1, %2\n\ts_waitcnt lgkmcnt(0)" : "=&v"(r) : "v"(off), "v"(1u) : "memory");
  return __builtin_amdgcn_readfirstlane(r);
}
__device__ __forceinline__ void ctl_wait_ge(int off, unsigned need) {
#pragma nounroll
  for (int i = 0; i < SPIN_CAP; ++i) {
    if (ctl_read(off) >= need) break;
    __builtin_amdgcn_s_sleep(1);
  }
}
__device__ __forceinline__ void wait_full(unsigned& have, unsigned need) {
#pragma nounroll
  for (int i = 0; have < need && i < SPIN_CAP; ++i) {
    have = ctl_read(L_FULL);
    if (have < need) __builtin_amdgcn_s_sleep(1);
  }
}

typedef __amdgpu_buffer_rsrc_t rsrc_t;
__device__ __forceinline__ rsrc_t mk_rsrc(const void* p, unsigned bytes) {
  return __builtin_amdgcn_make_buffer_rsrc(const_cast<void*>(p), 0, bytes, 0x00020000);
}
template <int O0, int O1>
__device__ __forceinline__ void poll2(const void* base, int voff, v4u& a, v4u& b) {
  asm volatile("global_load_dwordx4 %0, %2, %3 offset:%4 sc1\n\tglobal_load_dwordx4 %1, %2, %3 offset:%5 sc1\n\ts_waitcnt vmcnt(0)"
               : "=&v"(a), "=&v"(b) : "v"(voff), "s"(base), "i"(O0), "i"(O1) : "memory");
}
template <int O0, int O1, int O2>
__device__ __forceinline__ void poll3(const void* base, int voff, int voff2, v4u& a, v4u& b, v4u& c) {
  asm volatile("global_load_dwordx4 %0, %3, %5 offset:%6 sc1\n\tglobal_load_dwordx4 %1, %3, %5 offset:%7 sc1\n\t"
               "global_load_dwordx4 %2, %4, %5 offset:%8 sc1\n\ts_waitcnt vmcnt(0)"
               : "=&v"(a), "=&v"(b), "=&v"(c) : "v"(voff), "v"(voff2), "s"(base), "i"(O0), "i"(O1), "i"(O2) : "memory");
}
__device__ __forceinline__ void poll4(const void* base, int voff, v4u (&a)[4]) {
  asm volatile("global_load_dwordx4 %0, %4, %5 offset:-4096 sc1\n\tglobal_load_dwordx4 %1, %4, %5 offset:-2048 sc1\n\t"
               "global_load_dwordx4 %2, %4, %5 sc1\n\tglobal_load_dwordx4 %3, %4, %5 offset:2048 sc1\n\ts_waitcnt vmcnt(0)"
               : "=&v"(a[0]), "=&v"(a[1]), "=&v"(a[2]), "=&v"(a[3]) : "v"(voff), "s"(base) : "memory");
}
__device__ __forceinline__ void st_pair(rsrc_t r, int voff, int soff, float a, float b, unsigned tag) {
  v4u v = {__float_as_uint(a), tag, __float_as_uint(b), tag};
  __builtin_amdgcn_raw_buffer_store_b128(v, r, voff, soff, 16);
}
__device__ __forceinline__ bool tags_ok(v4u v, unsigned tag) { return v.y == tag && v.w == tag; }

struct Diag {
  unsigned sel[4];
  __device__ __forceinline__ void init(int c) {
    const unsigned src = c < 8 ? 0x0100u : 0x0302u;
    const unsigned one = (c & 1) ? ((src << 16) | 0x0c0cu) : (0x0c0c0000u | src);
#pragma unroll
    for (int d = 0; d < 4; ++d) sel[d] = (((c & 7) >> 1) == d) ? one : 0x0c0c0c0cu;
  }
  __device__ __forceinline__ bf16x8 frag(unsigned packed) const {
    v4u f;
#pragma unroll
    for (int d = 0; d < 4; ++d) f[d] = __builtin_amdgcn_perm(0u, packed, sel[d]);
    return __builtin_bit_cast(bf16x8, f);
  }
};
__device__ __forceinline__ v4f mfma(v4u a, bf16x8 b, v4f c) {
  return __builtin_amdgcn_mfma_f32_16x16x32_bf16(__builtin_bit_cast(bf16x8, a), b, c, 0, 0, 0);
}

#define STAMP(i) do { } while (0)

struct Loader {
  unsigned hist[INFL / 4];
  unsigned freeto;
  __device__ __forceinline__ void need_space(unsigned n) {
    if (n < freeto) return;
#pragma nounroll
    for (int i = 0; i < SPIN_CAP; ++i) {
      v4u d;
      asm volatile("ds_read_b128 %0, %1\n\ts_waitcnt lgkmcnt(0)" : "=v"(d) : "v"(L_DONE) : "memory");
      freeto = __builtin_amdgcn_readfirstlane(min(min(d.x, d.y), min(d.z, d.w))) + NS;
      if (n < freeto) break;
      __builtin_amdgcn_s_sleep(1);
    }
  }
  __device__ __forceinline__ void issued(unsigned n) {
    asm volatile("s_waitcnt vmcnt(%0)" :: "i"(INFL) : "memory");
    asm volatile("ds_write_b32 %0, %1" :: "v"(L_FULL), "v"(hist[0] + 1) : "memory");
#pragma unroll
    for (int i = 0; i + 1 < INFL / 4; ++i) hist[i] = hist[i + 1];
    hist[INFL / 4 - 1] = n;
  }
};
#define DMA(rs, slot, i, voff, soff, aux) \
  __builtin_amdgcn_raw_ptr_buffer_load_lds(rs, (LAS void*)(size_t)(L_RING + ((slot) % NS) * 4096 + (i) * 1024), 16, voff, soff, 0, aux)
#define DMA_LIN(rs, slot, i, voff, soff, aux) \
  __builtin_amdgcn_raw_ptr_buffer_load_lds(rs, (LAS void*)(size_t)(L_RING + ((slot) % NS) * 4096), 16, voff, soff, (i) * 1024, aux)

__global__ void __launch_bounds__(320, 1)
fused_decode(const u16* __restrict__ x, const u16* __restrict__ Kc, const u16* __restrict__ Vc, const u16* __restrict__ wq,
             const u16* __restrict__ wo, const u16* __restrict__ wg, const u16* __restrict__ wu, const u16* __restrict__ wd,
             u16* __restrict__ out, unsigned char* ws) {
  extern __shared__ __attribute__((aligned(16))) unsigned char smem[];
  const int lane = threadIdx.x & 63, w = __builtin_amdgcn_readfirstlane(threadIdx.x >> 6);
  const int g = lane >> 4, c = lane & 15;
  const int b = blockIdx.x, h = (b & 7) * 4 + (b >> 6), s = (b >> 3) & 7;
  const bool ffn = h < NFFN;
  const rsrc_t rws = mk_rsrc(ws, WS_END);

  if (w == 4) {
    __builtin_amdgcn_s_setprio(3);
    STAMP(0);
    Loader ld;
    ld.freeto = NS;
    const rsrc_t rq = mk_rsrc(wq, D * H * DH * 2);
    const int vq = (80 * s + g) * (H * DH * 2) + h * 256 + c * 16;
#pragma unroll
    for (int n = 0; n < INFL / 4; ++n) {
#pragma unroll
      for (int i = 0; i < 4; ++i) DMA(rq, n, i, vq, (4 * n + i) * 4 * (H * DH * 2), W1_AUX);
      ld.hist[n] = n;
    }
    __builtin_amdgcn_s_barrier();
#pragma unroll
    for (int n = INFL / 4; n < 5; ++n) {
#pragma unroll
      for (int i = 0; i < 4; ++i) DMA(rq, n, i, vq, (4 * n + i) * 4 * (H * DH * 2), W1_AUX);
      ld.issued(n);
    }
    if (ffn) {
      const rsrc_t rg = mk_rsrc(wg, D * F * 2), ru = mk_rsrc(wu, D * F * 2);
      const int vg = (80 * s + (lane >> 3)) * (F * 2) + h * 128 + (lane & 7) * 16;
#pragma unroll
      for (int n = 5; n < 10; ++n) {
#pragma unroll
        for (int i = 0; i < 4; ++i) DMA((i & 1) ? ru : rg, n, i, vg, (2 * (n - 5) + (i >> 1)) * 8 * (F * 2), W1_AUX);
        ld.issued(n);
      }
    }
    const int vl = lane * 16;
    const size_t kvoff = ((size_t)h * SK + s * 2048) * DH;
#pragma nounroll
    for (int ph = 0; ph < 2; ++ph) {
      const rsrc_t rk = mk_rsrc((ph ? Vc : Kc) + kvoff, 2048 * DH * 2);
      const int n0 = ph ? SLOT_V : SLOT_K;
#pragma nounroll
      for (int t = 0; t < 128; ++t) {
        const unsigned n = n0 + t;
        ld.need_space(n);
        DMA_LIN(rk, n, 0, vl, t * 4096, KV_AUX); DMA_LIN(rk, n, 1, vl, t * 4096, KV_AUX);
        DMA_LIN(rk, n, 2, vl, t * 4096, KV_AUX); DMA_LIN(rk, n, 3, vl, t * 4096, KV_AUX);
        ld.issued(n);
      }
      STAMP(1 + ph);
    }
    int vw[5];
#pragma unroll
    for (int i = 0; i < 5; ++i) { const int f = i * 64 + lane; vw[i] = (f / 10) * (D * 2) + (f % 10) * 16 + s * 160; }
    const rsrc_t ro = mk_rsrc(wo + (size_t)h * DH * D, DH * D * 2);
#pragma unroll
    for (int n = 0; n < 5; ++n) {
      ld.need_space(SLOT_W2 + n);
#pragma unroll
      for (int i = 0; i < 4; ++i) DMA(ro, SLOT_W2 + n, i, vw[(4 * n + i) % 5], ((4 * n + i) / 5) * 32 * (D * 2), W2_AUX);
      ld.issued(SLOT_W2 + n);
    }
    if (ffn) {
      const rsrc_t rd = mk_rsrc(wd + (size_t)h * 64 * D, 64 * D * 2);
#pragma unroll
      for (int n = 0; n < 3; ++n) {
        ld.need_space(SLOT_W2 + 5 + n);
#pragma unroll
        for (int i = 0; i < 4; ++i) DMA(rd, SLOT_W2 + 5 + n, i, vw[(4 * n + i) % 5], ((4 * n + i) / 5) * 32 * (D * 2), W2_AUX);
        ld.issued(SLOT_W2 + 5 + n);
      }
    }
#pragma unroll
    for (int k = INFL / 4 - 1; k >= 0; --k) {
      asm volatile("s_waitcnt vmcnt(%0)" :: "i"(4 * k) : "memory");
      asm volatile("ds_write_b32 %0, %1" :: "v"(L_FULL), "v"(k ? ld.hist[INFL / 4 - 1 - k] + 1 : 1000u) : "memory");
    }
    STAMP(3);
    if (h >= 10) return;
    const unsigned tag = ctl_read(L_TAG);
    const int col = 80 * s + 8 * h + 4 * (lane & 1);
    const v2u xr = *(const v2u*)(x + col);
    const int vy = WS_X3 + (s * 32 + (lane >> 1)) * 640 + h * 64 + (lane & 1) * 32;
    v4u a, bb;
#pragma nounroll
    for (int i = 0; i < SPIN_CAP; ++i) {
      poll2<0, 16>(ws, vy, a, bb);
      if (__all(tags_ok(a, tag) && tags_ok(bb, tag))) break;
      asm volatile("s_sleep 1" ::: "memory");
    }
    STAMP(4);
    float y[4] = {__uint_as_float(a.x), __uint_as_float(a.z), __uint_as_float(bb.x), __uint_as_float(bb.z)};
#pragma unroll
    for (int i = 0; i < 4; ++i) { y[i] += dpp<XOR2>(y[i]); y[i] += dpp<ROR4>(y[i]); y[i] += dpp<ROR8>(y[i]); y[i] = rows_sum(y[i]); }
    if (lane < 2) {
      v2u o;
      o.x = f2bf(bf2f(xr.x & 0xffffu) + y[0]) | (f2bf(bf2f(xr.x >> 16) + y[1]) << 16);
      o.y = f2bf(bf2f(xr.y & 0xffffu) + y[2]) | (f2bf(bf2f(xr.y >> 16) + y[3]) << 16);
      *(v2u*)(out + col) = o;
    }
    if (b == 0 && lane == 0) __hip_atomic_store((unsigned*)(ws + WS_EPOCH), tag, __ATOMIC_RELAXED, __HIP_MEMORY_SCOPE_AGENT);
    STAMP(5);
    return;
  }

  if (w == 0 && lane < 12) lds_st<unsigned>(L_DONE + lane * 4, 0u);
  asm volatile("s_waitcnt lgkmcnt(0)" ::: "memory");
  __builtin_amdgcn_s_barrier();
  unsigned xv[5];
#pragma unroll
  for (int i = 0; i < 5; ++i) xv[i] = ((const unsigned*)x)[lane + 64 * i];
  unsigned xq[5], xg[5];
#pragma unroll
  for (int t = 0; t < 5; ++t) { xq[t] = x[80 * s + 20 * w + 4 * t + g]; xg[t] = x[80 * s + 16 * t + 8 * (w >> 1) + 4 * (w & 1) + g]; }
  const unsigned tag = __hip_atomic_load((const unsigned*)(ws + WS_EPOCH), __ATOMIC_RELAXED, __HIP_MEMORY_SCOPE_AGENT) + 1u;
  if (w == 0) lds_st<unsigned>(L_TAG, tag);

  Diag dg;
  dg.init(c);
  float sum = 0.f;
#pragma unroll
  for (int i = 0; i < 5; ++i) sum += bf2f(xv[i] & 0xffffu) + bf2f(xv[i] >> 16);
  const float mean = wave_sum(sum) * (1.f / D);
  float var = 0.f;
#pragma unroll
  for (int i = 0; i < 5; ++i) { const float d0 = bf2f(xv[i] & 0xffffu) - mean, d1 = bf2f(xv[i] >> 16) - mean; var += d0 * d0 + d1 * d1; }
  var = wave_sum(var) * (1.f / D);
  const float mb = rbf(mean), den = rbf(sqrtf(rbf(rbf(var) + 1e-5f)));
#pragma unroll
  for (int t = 0; t < 5; ++t) { xq[t] = f2bf(rbf(bf2f(xq[t]) - mb) / den); xg[t] = f2bf(rbf(bf2f(xg[t]) - mb) / den); }

  unsigned full = 0;
  {
    v4f aq = {0.f, 0.f, 0.f, 0.f}, ag = aq;
    wait_full(full, 5);
#pragma unroll
    for (int t = 0; t < 5; ++t) aq = mfma(lds_ld<v4u>(L_RING + (5 * w + t) * 1024 + lane * 16), dg.frag(xq[t]), aq);
    if (ffn) {
      wait_full(full, 10);
      const int ao = L_RING + 5 * 4096 + (w >> 1) * 2048 + (c >> 3) * 1024 + (4 * (w & 1) + g) * 128 + (c & 7) * 16;
#pragma unroll
      for (int t = 0; t < 5; ++t) ag = mfma(lds_ld<v4u>(ao + t * 4096), dg.frag(xg[t]), ag);
    }
    if (c < 8) { lds_st<v4f>(L_RED + w * 1024 + (g * 8 + c) * 16, aq); lds_st<v4f>(L_RED + w * 1024 + 512 + (g * 8 + c) * 16, ag); }
  }
  ctl_write(L_DONE + w * 4, SLOT_K + w);
  if (w == 0) STAMP(8);
  if (ctl_arrive(L_CNT, lane) == 3) {
    v4f p = lds_ld<v4f>(L_RED + lane * 16);
#pragma unroll
    for (int i = 1; i < 4; ++i) p += lds_ld<v4f>(L_RED + i * 1024 + lane * 16);
    const int o = WS_X1 + (h * 8 + s) * 2048 + lane * 32;
    st_pair(rws, o, 0, p.x, p.y, tag);
    st_pair(rws, o + 16, 0, p.z, p.w, tag);
  }
  if (w < 2 || ffn) {
    const int o = WS_X1 + (h * 8 + (lane >> 5) * 4) * 2048 + (64 * w + 2 * (lane & 31)) * 8;
    v4u a[4];
#pragma nounroll
    for (int i = 0; i < SPIN_CAP; ++i) {
      poll4(ws, o + 4096, a);
      if (__all(tags_ok(a[0], tag) && tags_ok(a[1], tag) && tags_ok(a[2], tag) && tags_ok(a[3], tag))) break;
      asm volatile("s_sleep 2" ::: "memory");
    }
    const float v0 = halves_sum((__uint_as_float(a[0].x) + __uint_as_float(a[1].x)) + (__uint_as_float(a[2].x) + __uint_as_float(a[3].x)));
    const float v1 = halves_sum((__uint_as_float(a[0].z) + __uint_as_float(a[1].z)) + (__uint_as_float(a[2].z) + __uint_as_float(a[3].z)));
    const int P = (64 * w + 2 * (lane & 31)) & 127, e = (P & 96) + 8 * (P & 3) + ((P >> 2) & 7);
    if (lane < 32) {
      if (w < 2) { lds_st<u16>(L_Q + e * 2, (u16)f2bf(v0)); lds_st<u16>(L_Q + (e + 8) * 2, (u16)f2bf(v1)); }
      else { lds_st<float>(L_GU + e * 4, rbf(v0)); lds_st<float>(L_GU + (e + 8) * 4, rbf(v1)); }
    }
  }
  ctl_arrive(L_CNT + 4, lane);
  ctl_wait_ge(L_CNT + 4, 4);
  if (w == 0) STAMP(9);
  bf16x8 qf[4];
#pragma unroll
  for (int i = 0; i < 4; ++i) qf[i] = __builtin_bit_cast(bf16x8, lds_ld<v4u>(L_Q + (((4 * i + g) ^ c) * 16)));
  unsigned hv[4] = {0u, 0u, 0u, 0u};
  if (ffn) {
#pragma unroll
    for (int t = 0; t < 4; ++t) {
      const float gt = lds_ld<float>(L_GU + (16 * w + 4 * t + g) * 4), up = lds_ld<float>(L_GU + 256 + (16 * w + 4 * t + g) * 4);
      hv[t] = f2bf(rbf(gt / (1.f + __expf(-gt))) * up);
    }
  }

  const int sc = L_SC + w * 2048;
#pragma nounroll
  for (int it = 0; it < 32; ++it) {
    const unsigned n = SLOT_K + 4 * it + w;
    wait_full(full, n + 1);
    const int base = L_RING + (n % NS) * 4096 + c * 256;
    v4u kf[4];
#pragma unroll
    for (int i = 0; i < 4; ++i) kf[i] = lds_ld<v4u>(base + (((4 * i + g) ^ c) * 16));
    ctl_write(L_DONE + w * 4, n + 4);
    v4f d0 = {0.f, 0.f, 0.f, 0.f}, d1 = d0;
    d0 = mfma(kf[0], qf[0], d0); d1 = mfma(kf[1], qf[1], d1);
    d0 = mfma(kf[2], qf[2], d0); d1 = mfma(kf[3], qf[3], d1);
    d0 += d1;
    const float dv = (c & 2) ? ((c & 1) ? d0.w : d0.z) : ((c & 1) ? d0.y : d0.x);
    const float sv = rbf(rbf(dv) * 0.088388347648318440f) * 1.4426950408889634f;
    if ((c >> 2) == g) lds_st<float>(sc + (it * 16 + c) * 4, sv);
  }
  if (w == 0) STAMP(10);
  float mw, lw;
  {
    v4f s0 = lds_ld<v4f>(sc + lane * 16), s1 = lds_ld<v4f>(sc + 1024 + lane * 16);
    mw = wave_max(fmaxf(fmaxf(fmaxf(s0.x, s0.y), fmaxf(s0.z, s0.w)), fmaxf(fmaxf(s1.x, s1.y), fmaxf(s1.z, s1.w))));
    v4u p0, p1;
    float l = 0.f;
#pragma unroll
    for (int k = 0; k < 4; ++k) {
      const float a = __builtin_amdgcn_exp2f(s0[k] - mw), bq = __builtin_amdgcn_exp2f(s1[k] - mw);
      l += a + bq;
      const unsigned ah = f2bf(a), bh = f2bf(bq);
      p0[k] = ah | (f2bf(a - bf2f(ah)) << 16);
      p1[k] = bh | (f2bf(bq - bf2f(bh)) << 16);
    }
    lw = wave_sum(l);
    lds_st<v4u>(sc + lane * 16, p0); lds_st<v4u>(sc + 1024 + lane * 16, p1);
  }
  v4f o0 = {0.f, 0.f, 0.f, 0.f}, o1 = o0;
#pragma nounroll
  for (int it = 0; it < 32; ++it) {
    const unsigned n = SLOT_V + 4 * it + w;
    const v4u p = lds_ld<v4u>(sc + (it * 16 + 4 * g) * 4);
    bf16x8 pf[4];
#pragma unroll
    for (int t = 0; t < 4; ++t) pf[t] = dg.frag(p[t]);
    wait_full(full, n + 1);
    const int base = L_RING + (n % NS) * 4096 + g * 1024 + c * 16;
    v4u vf[4];
#pragma unroll
    for (int t = 0; t < 4; ++t) vf[t] = lds_ld<v4u>(base + t * 256);
    ctl_write(L_DONE + w * 4, n + 4);
    o0 = mfma(vf[0], pf[0], o0); o1 = mfma(vf[1], pf[1], o1);
    o0 = mfma(vf[2], pf[2], o0); o1 = mfma(vf[3], pf[3], o1);
  }
  if (w == 0) STAMP(11);
  o0 += o1;
#pragma unroll
  for (int k = 0; k < 4; ++k) o0[k] += dpp<ROR8>(o0[k]);
  if (c < 8) lds_st<v4f>(L_RED + w * 1024 + (g * 8 + c) * 16, o0);
  if (lane == 0) lds_st<v2f>(L_ML + w * 8, v2f{mw, lw});
  if (ctl_arrive(L_CNT + 8, lane) == 3) {
    const v4f ml0 = lds_ld<v4f>(L_ML), ml1 = lds_ld<v4f>(L_ML + 16);
    const float M = fmaxf(fmaxf(ml0.x, ml0.z), fmaxf(ml1.x, ml1.z));
    const float f0 = __builtin_amdgcn_exp2f(ml0.x - M), f1 = __builtin_amdgcn_exp2f(ml0.z - M);
    const float f2 = __builtin_amdgcn_exp2f(ml1.x - M), f3 = __builtin_amdgcn_exp2f(ml1.z - M);
    const v2f a = f0 * lds_ld<v2f>(L_RED + lane * 8) + f1 * lds_ld<v2f>(L_RED + 1024 + lane * 8) +
                  f2 * lds_ld<v2f>(L_RED + 2048 + lane * 8) + f3 * lds_ld<v2f>(L_RED + 3072 + lane * 8);
    const int o = WS_X2 + (h * 8 + s) * X2_STRIDE;
    st_pair(rws, o + lane * 16, 0, a.x, a.y, tag);
    if (lane == 0) st_pair(rws, o + 1024, 0, M, f0 * ml0.y + f1 * ml0.w + f2 * ml1.y + f3 * ml1.w, tag);
  }
  if (w == 0) STAMP(12);
  unsigned ov[8];
  {
    const int o = WS_X2 + (h * 8 + (c & 7)) * X2_STRIDE;
    const int po = o + (32 * w + 8 * g + 4 * (c >> 3)) * 8;
    v4u a, bq, ml;
#pragma nounroll
    for (int i = 0; i < SPIN_CAP; ++i) {
      poll3<0, 16, 1024>(ws, po, o, a, bq, ml);
      if (__all(tags_ok(a, tag) && tags_ok(bq, tag) && tags_ok(ml, tag))) break;
      asm volatile("s_sleep 1" ::: "memory");
    }
    const float mp = __uint_as_float(ml.x);
    float M = fmaxf(mp, dpp<XOR1>(mp)); M = fmaxf(M, dpp<XOR2>(M)); M = fmaxf(M, dpp<HMIR>(M));
    const float f = __builtin_amdgcn_exp2f(mp - M);
    float v[5] = {f * __uint_as_float(a.x), f * __uint_as_float(a.z), f * __uint_as_float(bq.x), f * __uint_as_float(bq.z), f * __uint_as_float(ml.z)};
#pragma unroll
    for (int k = 0; k < 5; ++k) { v[k] += dpp<XOR1>(v[k]); v[k] += dpp<XOR2>(v[k]); v[k] += dpp<HMIR>(v[k]); }
#pragma unroll
    for (int k = 0; k < 4; ++k) {
      const float mine = v[k] / v[4], other = dpp<ROR8>(mine);
      ov[2 * k] = f2bf(c < 8 ? mine : other);
      ov[2 * k + 1] = f2bf(c < 8 ? other : mine);
    }
  }
  if (w == 0) STAMP(13);
  {
    const int wbase = L_RING + (SLOT_W2 % NS) * 4096 + c * 16;
    v4f y0 = {0.f, 0.f, 0.f, 0.f}, y1 = y0;
    wait_full(full, 1000);
#pragma unroll
    for (int t = 0; t < 8; t += 2) {
      y0 = mfma(lds_ld<v4u>(wbase + (32 * w + 4 * t + 2 * g) * 160), dg.frag(ov[t]), y0);
      y1 = mfma(lds_ld<v4u>(wbase + (32 * w + 4 * t + 2 * g + 1) * 160), dg.frag(ov[t + 1]), y1);
    }
    if (ffn) {
#pragma unroll
      for (int t = 0; t < 4; t += 2) {
        y0 = mfma(lds_ld<v4u>(wbase + 5 * 4096 + (16 * w + 4 * t + g) * 160), dg.frag(hv[t]), y0);
        y1 = mfma(lds_ld<v4u>(wbase + 5 * 4096 + (16 * w + 4 * t + 4 + g) * 160), dg.frag(hv[t + 1]), y1);
      }
    }
    y0 += y1;
    if (c < 8) {
#pragma unroll
      for (int k = 0; k < 4; ++k) lds_st<float>(L_SC + w * 512 + (8 * (4 * g + k) + c) * 4, y0[k]);
    }
  }
  if (ctl_arrive(L_CNT + 12, lane) == 3 && lane < 40) {
    const v2f a = (lds_ld<v2f>(L_SC + lane * 8) + lds_ld<v2f>(L_SC + 512 + lane * 8)) + (lds_ld<v2f>(L_SC + 1024 + lane * 8) + lds_ld<v2f>(L_SC + 1536 + lane * 8));
    st_pair(rws, WS_X3 + (s * 32 + h) * 640 + lane * 16, 0, a.x, a.y, tag);
  }
  if (w == 0) STAMP(14);
}

}

extern "C" void kernel_launch(void* const* d_in, const int* in_sizes, int n_in, void* d_out, int out_size, void* d_ws,
                              size_t ws_size, hipStream_t stream) {
  (void)in_sizes; (void)n_in; (void)out_size; (void)ws_size;
  (void)hipFuncSetAttribute((const void*)fused_decode, hipFuncAttributeMaxDynamicSharedMemorySize, LDS_BYTES);
  fused_decode<<<256, 320, LDS_BYTES, stream>>>((const u16*)d_in[0], (const u16*)d_in[1], (const u16*)d_in[2], (const u16*)d_in[3],
                                               (const u16*)d_in[4], (const u16*)d_in[6], (const u16*)d_in[7], (const u16*)d_in[8],
                                               (u16*)d_out, (unsigned char*)d_ws);
}
```

```cpp
#include <hip/hip_runtime.h>
#include <stdint.h>

namespace {

typedef unsigned short u16;
typedef unsigned long long u64;
typedef unsigned v4u __attribute__((ext_vector_type(4)));
typedef float v4f __attribute__((ext_vector_type(4)));
typedef float v2f __attribute__((ext_vector_type(2)));
typedef __bf16 bf16x8 __attribute__((ext_vector_type(8)));
typedef __bf16 bf16x2 __attribute__((ext_vector_type(2)));
#define LAS __attribute__((address_space(3)))

constexpr int D = 640, H = 32, SK = 16384, DH = 128, F = 1728;

constexpr int DT = 3450;
constexpr int T0 = 40;
constexpr int LO = 18, HI = 20;
constexpr int UP = 400, DOWN = 100;
constexpr int LOWKEEP = 24, MIDKEEP = 36, CAP = 52;
constexpr int SKEWMAX = 200;
constexpr int QWAIT = 22, OWAIT = 24, YWAIT = 16;
constexpr int T_PUT = 32, T_GET = 50, G_PUT = 60, G_GET = 84, M_GET = 24;
constexpr int PS0 = 10, PS1 = 11, PS2 = 21, PI0 = 12, PI1 = 14, PT = 13;
constexpr int PJ[6] = {15, 16, 20, 17, 19, 18};

constexpr int NS = 36;
constexpr int NG = 6, SLOT_V = NG + 128, SLOT_W = NG + 256, SLOT_END = 1000;
constexpr int NFFN = F / 64;
static_assert(NS % 2 == 0 && SLOT_W % NS + 9 <= NS, "weight slices must not wrap around the ring");

constexpr int L_RING = 0;
constexpr int L_SC = NS * 4096;
constexpr int L_RED = L_SC + 8192;
constexpr int L_Q = L_RED + 3 * 2048;
constexpr int L_H = L_Q + 256;
constexpr int L_M = L_H + 256;
constexpr int L_L = L_M + 16;
constexpr int L_DONE = L_L + 16;
constexpr int L_FULL = L_DONE + 16;
constexpr int L_CNT = L_FULL + 4;
constexpr int C_Q = L_CNT, C_QRDY = L_CNT + 4, C_M = L_CNT + 8, C_G = L_CNT + 12, C_GRDY = L_CNT + 16, C_O = L_CNT + 20, C_Y = L_CNT + 24;
constexpr int C_QGO = L_CNT + 28, C_OGO = L_CNT + 32, C_YGO = L_CNT + 36, C_KGO = L_CNT + 40, C_L = L_CNT + 44;
constexpr int L_LEAD = L_CNT + 48;
constexpr int N_CTL = (L_LEAD + 4 - L_DONE) / 4;
constexpr int L_ORG = L_LEAD + 4, L_SCALE = L_ORG + 4;
constexpr int LDS_BYTES = L_SCALE + 4;
static_assert(LDS_BYTES <= 163840 && L_RING == 0, "LDS");

constexpr int WS_EPOCH = 0;
constexpr int WS_XQ = 4096;
constexpr int XG_STRIDE = 1152;
constexpr int WS_XG = WS_XQ + 256 * 1024;
constexpr int WS_XO = WS_XG + 256 * XG_STRIDE;
constexpr int WS_XY = WS_XO + 256 * 1024;
constexpr int WS_XT = WS_XY + 256 * 640;
constexpr int WS_END = WS_XT + 8 * 1024;
constexpr int SPIN_CAP = 1 << 20;

__device__ __forceinline__ float bf2f(unsigned x) { return __uint_as_float(x << 16); }
__device__ __forceinline__ unsigned f2bf(float x) { return __builtin_bit_cast(u16, (__bf16)x); }
__device__ __forceinline__ float rbf(float x) { return (float)(__bf16)x; }

template <int CTRL> __device__ __forceinline__ float dpp(float x) {
  return __builtin_bit_cast(float, __builtin_amdgcn_mov_dpp(__builtin_bit_cast(int, x), CTRL, 0xf, 0xf, true));
}
constexpr int XOR1 = 0xB1, XOR2 = 0x4E, HMIR = 0x141, ROR4 = 0x124, ROR8 = 0x128;
__device__ __forceinline__ float rows_sum(float x) {
  auto s = __builtin_amdgcn_permlane16_swap(__float_as_uint(x), __float_as_uint(x), false, false);
  x = __uint_as_float(s[0]) + __uint_as_float(s[1]);
  auto t = __builtin_amdgcn_permlane32_swap(__float_as_uint(x), __float_as_uint(x), false, false);
  return __uint_as_float(t[0]) + __uint_as_float(t[1]);
}
__device__ __forceinline__ float rows_max(float x) {
  auto s = __builtin_amdgcn_permlane16_swap(__float_as_uint(x), __float_as_uint(x), false, false);
  x = fmaxf(__uint_as_float(s[0]), __uint_as_float(s[1]));
  auto t = __builtin_amdgcn_permlane32_swap(__float_as_uint(x), __float_as_uint(x), false, false);
  return fmaxf(__uint_as_float(t[0]), __uint_as_float(t[1]));
}
__device__ __forceinline__ float wave_sum(float x) {
  x += dpp<XOR1>(x); x += dpp<XOR2>(x); x += dpp<HMIR>(x); x += dpp<ROR8>(x);
  return rows_sum(x);
}
__device__ __forceinline__ float wave_max(float x) {
  x = fmaxf(x, dpp<XOR1>(x)); x = fmaxf(x, dpp<XOR2>(x)); x = fmaxf(x, dpp<HMIR>(x)); x = fmaxf(x, dpp<ROR8>(x));
  return rows_max(x);
}

template <typename T> __device__ __forceinline__ T lds_ld(int off) { return *(const LAS T*)(size_t)off; }
template <typename T> __device__ __forceinline__ void lds_st(int off, T v) { *(LAS T*)(size_t)off = v; }

__device__ __forceinline__ unsigned ctl_read(int off) {
  unsigned r;
  asm volatile("ds_read_b32 %0, %1\n\ts_waitcnt lgkmcnt(0)" : "=v"(r) : "v"(off) : "memory");
  return __builtin_amdgcn_readfirstlane(r);
}
__device__ __forceinline__ void ctl_write(int off, unsigned v) {
  asm volatile("s_waitcnt lgkmcnt(0)\n\tds_write_b32 %0, %1" :: "v"(off), "v"(v) : "memory");
}
__device__ __forceinline__ unsigned ctl_arrive(int off) {
  unsigned r, o;
  asm volatile("s_waitcnt lgkmcnt(0)\n\ts_mov_b64 exec, 1\n\tds_add_rtn_u32 %0, %2, %3\n\ts_mov_b64 exec, -1\n\t"
               "s_waitcnt lgkmcnt(0)\n\tv_readfirstlane_b32 %1, %0"
               : "=&v"(r), "=s"(o) : "v"(off), "v"(1u) : "memory");
  return o;
}
template <int SLEEP> __device__ __forceinline__ int ctl_wait_ge(int off, int need) {
  int v, got, left;
  asm volatile("s_mov_b32 %2, %5\n"
               "CW_top%=:\n\tds_read_b32 %0, %3\n\ts_sub_u32 %2, %2, 1\n\ts_cbranch_scc1 CW_out%=\n\t"
               "s_waitcnt lgkmcnt(0)\n\tv_readfirstlane_b32 %1, %0\n\ts_cmp_ge_i32 %1, %4\n\ts_cbranch_scc1 CW_out%=\n\t"
               "s_sleep %6\n\ts_branch CW_top%=\n"
               "CW_out%=:\n\ts_waitcnt lgkmcnt(0)"
               : "=&v"(v), "=&s"(got), "=&s"(left) : "v"(off), "s"(need), "i"(SPIN_CAP), "i"(SLEEP) : "memory", "scc");
  return got;
}
__device__ __forceinline__ void wait_full(int& have, int need) {
  if (have < need) have = ctl_wait_ge<1>(L_FULL, need);
}

__device__ __forceinline__ unsigned read_tag(const unsigned char* ws) {
  return *(const __attribute__((address_space(4))) unsigned*)(size_t)(ws + WS_EPOCH) + 1u;
}
typedef __amdgpu_buffer_rsrc_t rsrc_t;
__device__ __forceinline__ rsrc_t mk_rsrc(const void* p, unsigned bytes) {
  return __builtin_amdgcn_make_buffer_rsrc(const_cast<void*>(p), 0, bytes, 0x00020000);
}
template <int AUX = 16>
__device__ __forceinline__ void st_pair(rsrc_t r, int voff, float a, float b, unsigned tag) {
  v4u v = {__float_as_uint(a), tag, __float_as_uint(b), tag};
  __builtin_amdgcn_raw_buffer_store_b128(v, r, voff, 0, AUX);
}
__device__ __forceinline__ u64 missing(v4u a, unsigned tag) {
  return __builtin_amdgcn_ballot_w64(a.y != tag) | __builtin_amdgcn_ballot_w64(a.w != tag);
}
#define MASKED(loads) "s_mov_b64 exec, %[m]\n\t" loads "s_mov_b64 exec, -1\n\ts_waitcnt vmcnt(0)"
__device__ __forceinline__ bool wait_pair(const void* base, int voff, unsigned tag, v4u& a, int tries) {
  a = v4u{0u, ~tag, 0u, ~tag};
#pragma nounroll
  for (int i = 0; i < tries; ++i) {
    const u64 m = missing(a, tag);
    if (m == 0) return true;
    if (i) __builtin_amdgcn_s_sleep(12);
    asm volatile(MASKED("global_load_dwordx4 %0, %[o], %[b] sc1 nt\n\t") : "+v"(a) : [m] "s"(m), [o] "v"(voff), [b] "s"(base) : "memory");
  }
  return missing(a, tag) == 0;
}
__device__ __forceinline__ void wait_pairs8(const void* base, int voff, unsigned tag, v4u (&a)[8]) {
#pragma unroll
  for (int k = 0; k < 8; ++k) a[k] = v4u{0u, ~tag, 0u, ~tag};
#pragma nounroll
  for (int i = 0; i < SPIN_CAP; ++i) {
    u64 m = 0;
#pragma unroll
    for (int k = 0; k < 8; ++k) m |= missing(a[k], tag);
    if (m == 0) break;
    if (i) __builtin_amdgcn_s_sleep(12);
    asm volatile(MASKED("global_load_dwordx4 %0, %[o], %[b] sc1 nt\n\tglobal_load_dwordx4 %1, %[o], %[b] offset:128 sc1 nt\n\t"
                        "global_load_dwordx4 %2, %[o], %[b] offset:256 sc1 nt\n\tglobal_load_dwordx4 %3, %[o], %[b] offset:384 sc1 nt\n\t"
                        "global_load_dwordx4 %4, %[o], %[b] offset:512 sc1 nt\n\tglobal_load_dwordx4 %5, %[o], %[b] offset:640 sc1 nt\n\t"
                        "global_load_dwordx4 %6, %[o], %[b] offset:768 sc1 nt\n\tglobal_load_dwordx4 %7, %[o], %[b] offset:896 sc1 nt\n\t")
                 : "+v"(a[0]), "+v"(a[1]), "+v"(a[2]), "+v"(a[3]), "+v"(a[4]), "+v"(a[5]), "+v"(a[6]), "+v"(a[7])
                 : [m] "s"(m), [o] "v"(voff), [b] "s"(base) : "memory");
  }
}
__device__ __forceinline__ void look2(const void* base, int voff, v4u& a, v4u& b) {
  asm volatile("global_load_dwordx4 %0, %2, %3 sc1\n\tglobal_load_dwordx4 %1, %2, %3 offset:16 sc1"
               : "=&v"(a), "=&v"(b) : "v"(voff), "s"(base) : "memory");
}
__device__ __forceinline__ void finish2(const void* base, int voff, unsigned tag, v4u& a, v4u& b) {
  asm volatile("s_waitcnt vmcnt(0)" : "+v"(a), "+v"(b) :: "memory");
#pragma nounroll
  for (int i = 0; i < SPIN_CAP; ++i) {
    const u64 m = missing(a, tag) | missing(b, tag);
    if (m == 0) break;
    asm volatile(MASKED("global_load_dwordx4 %0, %[o], %[b] sc1\n\tglobal_load_dwordx4 %1, %[o], %[b] offset:16 sc1\n\t")
                 : "+v"(a), "+v"(b) : [m] "s"(m), [o] "v"(voff), [b] "s"(base) : "memory");
  }
}
__device__ __forceinline__ void sum8x4(v4u a, v4u b, float (&v)[4]) {
  v[0] = __uint_as_float(a.x); v[1] = __uint_as_float(a.z); v[2] = __uint_as_float(b.x); v[3] = __uint_as_float(b.z);
#pragma unroll
  for (int k = 0; k < 4; ++k) v[k] += dpp<XOR1>(v[k]);
  __builtin_amdgcn_sched_barrier(0);
#pragma unroll
  for (int k = 0; k < 4; ++k) v[k] += dpp<XOR2>(v[k]);
  __builtin_amdgcn_sched_barrier(0);
#pragma unroll
  for (int k = 0; k < 4; ++k) v[k] += dpp<HMIR>(v[k]);
  __builtin_amdgcn_sched_barrier(0);
}
__device__ __forceinline__ float sum8(float v) { v += dpp<XOR1>(v); v += dpp<XOR2>(v); v += dpp<HMIR>(v); return v; }
template <int AREA> __device__ __forceinline__ v2f red_sum(int base) {
  return (lds_ld<v2f>(base + AREA) + lds_ld<v2f>(base + AREA + 512)) + (lds_ld<v2f>(base + AREA + 1024) + lds_ld<v2f>(base + AREA + 1536));
}

struct Diag {
  unsigned sel[4];
  __device__ __forceinline__ void init(int c) {
    const unsigned src = c < 8 ? 0x0100u : 0x0302u;
    const unsigned one = (c & 1) ? ((src << 16) | 0x0c0cu) : (0x0c0c0000u | src);
#pragma unroll
    for (int d = 0; d < 4; ++d) sel[d] = (((c & 7) >> 1) == d) ? one : 0x0c0c0c0cu;
  }
  __device__ __forceinline__ bf16x8 frag(unsigned packed) const {
    v4u f;
#pragma unroll
    for (int d = 0; d < 4; ++d) f[d] = __builtin_amdgcn_perm(0u, packed, sel[d]);
    return __builtin_bit_cast(bf16x8, f);
  }
};
__device__ __forceinline__ v4f mfma(v4u a, bf16x8 b, v4f c) {
  return __builtin_amdgcn_mfma_f32_16x16x32_bf16(__builtin_bit_cast(bf16x8, a), b, c, 0, 0, 0);
}

__device__ __forceinline__ v4u mk_srd(const void* p, unsigned bytes) {
  const unsigned long long a = (unsigned long long)p;
  return v4u{(unsigned)a, (unsigned)(a >> 32) & 0xffffu, bytes, 0x00020000u};
}

__device__ __forceinline__ int k_rotation(unsigned origin) { return (origin / (4 * DT)) & 31; }

struct Loader {
  int n;
  int freeto;
  unsigned due;
  int off;
  unsigned now;

  __device__ __forceinline__ void start(v4u rg, v4u ru, int voff) {
    int t0, t1, vt, so = 0;
    asm volatile(
        "s_mov_b32 %[t1], %[spincap]\n"
        "ST_wait%=:\n\t"
        "ds_read_b32 %[vt], %[ago]\n\t"
        "s_sub_u32 %[t1], %[t1], 1\n\t"
        "s_cbranch_scc1 ST_go%=\n\t"
        "s_waitcnt lgkmcnt(0)\n\t"
        "v_readfirstlane_b32 %[t0], %[vt]\n\t"
        "s_cmp_gt_i32 %[t0], 0\n\t"
        "s_cbranch_scc0 ST_wait%=\n"
        "ST_go%=:\n\t"
        "s_memrealtime vcc\n\t"
        "s_mov_b32 m0, 0\n\t"
        "s_waitcnt lgkmcnt(0)\n\t"
        "s_lshl_b32 %[due], vcc_lo, 8\n\t"
        "s_add_u32 %[due], %[due], %[lead]\n\t"
        "v_mov_b32 %[vt], %[due]\n\t"
        "ds_write_b32 %[aorg], %[vt]\n"
        "ST_spin%=:\n\t"
        "s_memrealtime vcc\n\t"
        "s_add_u32 %[t1], %[so], %[half]\n\t"
        "s_waitcnt lgkmcnt(0)\n\t"
        "s_lshl_b32 %[t0], vcc_lo, 8\n\t"
        "s_sub_u32 %[t0], %[t0], %[due]\n\t"
        "s_cmp_lt_i32 %[t0], 0\n\t"
        "s_cbranch_scc1 ST_spin%=\n\t"
        "buffer_load_dwordx4 %[v], %[rg], %[so] offen nt lds\n\t"
        "s_add_u32 m0, m0, 0x400\n\t"
        "s_add_u32 %[due], %[due], %[dt]\n\t"
        "buffer_load_dwordx4 %[v], %[ru], %[so] offen nt lds\n\t"
        "s_add_u32 m0, m0, 0x400\n\t"
        "s_add_u32 %[so], %[t1], %[half]\n\t"
        "buffer_load_dwordx4 %[v], %[rg], %[t1] offen nt lds\n\t"
        "s_add_u32 m0, m0, 0x400\n\t"
        "s_nop 0\n\t"
        "buffer_load_dwordx4 %[v], %[ru], %[t1] offen nt lds\n\t"
        "s_add_u32 m0, m0, 0x400\n\t"
        "s_cmp_lg_u32 m0, 0x5000\n\t"
        "s_cbranch_scc1 ST_spin%=\n\t"
        "s_sub_u32 %[due], %[due], %[dt]\n\t"
        "s_mov_b32 %[now], vcc_lo"
        : [due] "=&s"(due), [now] "=&s"(now), [so] "+s"(so), [t0] "=&s"(t0), [t1] "=&s"(t1), [vt] "=&v"(vt)
        : [rg] "s"(rg), [ru] "s"(ru), [v] "v"(voff), [ago] "v"(C_KGO), [aorg] "v"(L_ORG), [dt] "i"(DT), [lead] "i"(T0 << 8),
          [half] "i"(8 * F * 2), [spincap] "i"(SPIN_CAP)
        : "memory", "scc", "vcc");
  }

  __device__ __forceinline__ void stream(v4u srd, const int (&va)[4], const int (&vb)[4], int tab, int& pos, int& pair, int end, int dt, int room,
                                         int adone) {
    constexpr int ENC_HI = ((HI >> 4) << 22) | (HI & 15), ENC_LOW = ((LO >> 4) << 22) | (LO & 15);
    static_assert(LO <= LOWKEEP - 4, "the shallow path must not block");
    constexpr int ENC_MID = ((MIDKEEP - 4) >> 4 << 22) | ((MIDKEEP - 4) & 15);
    int t0, t1, so, vf = n - LOWKEEP / 4, vt;
    asm volatile(
        "v_readlane_b32 %[so], %[tab], %[pair]\n\t"
        "s_mov_b32 vcc_lo, %[now]\n\t"
        "s_add_u32 %[t1], %[due], %[off]\n"
        "LS_top%=:\n\t"
        "s_cmp_lt_i32 %[n], %[freeto]\n\t"
        "s_cbranch_scc0 LS_space%=\n"
        "LS_free%=:\n\t"
        "s_add_u32 %[t1], %[t1], %[dt]\n\t"
        "s_mov_b32 m0, %[pos]\n"
        "LS_spina%=:\n\t"
        "s_waitcnt lgkmcnt(0)\n\t"
        "s_lshl_b32 %[t0], vcc_lo, 8\n\t"
        "s_sub_u32 %[t0], %[t0], %[t1]\n\t"
        "s_cmp_lt_i32 %[t0], 0\n\t"
        "s_cbranch_scc1 LS_waita%=\n\t"
        "buffer_load_dwordx4 %[a0], %[srd], %[so] offen nt lds\n\t"
        "buffer_load_dwordx4 %[a1], %[srd], %[so] offen offset:1024 nt lds\n\t"
        "buffer_load_dwordx4 %[a2], %[srd], %[so] offen offset:2048 nt lds\n\t"
        "buffer_load_dwordx4 %[a3], %[srd], %[so] offen offset:3072 nt lds\n\t"
        "s_memrealtime vcc\n\t"
        "s_add_u32 %[t1], %[t1], %[dt]\n\t"
        "s_add_u32 m0, %[pos], 0x1000\n\t"
        "s_add_u32 %[pos], %[pos], 0x2000\n\t"
        "s_cmp_eq_u32 %[pos], %[ringend]\n\t"
        "s_cselect_b32 %[pos], 0, %[pos]\n\t"
        "v_add_u32 %[vf], 2, %[vf]\n"
        "LS_spinb%=:\n\t"
        "s_waitcnt lgkmcnt(0)\n\t"
        "s_lshl_b32 %[t0], vcc_lo, 8\n\t"
        "s_sub_u32 %[t0], %[t0], %[t1]\n\t"
        "s_cmp_lt_i32 %[t0], 0\n\t"
        "s_cbranch_scc1 LS_waitb%=\n\t"
        "s_getreg_b32 %[t0], hwreg(HW_REG_IB_STS)\n\t"
        "buffer_load_dwordx4 %[b0], %[srd], %[so] offen nt lds\n\t"
        "buffer_load_dwordx4 %[b1], %[srd], %[so] offen offset:1024 nt lds\n\t"
        "buffer_load_dwordx4 %[b2], %[srd], %[so] offen offset:2048 nt lds\n\t"
        "buffer_load_dwordx4 %[b3], %[srd], %[so] offen offset:3072 nt lds\n\t"
        "s_memrealtime vcc\n\t"
        "s_and_b32 %[t0], %[t0], 0xc0000f\n\t"
        "s_cmp_gt_u32 %[t0], %[enclow]\n\t"
        "s_cbranch_scc1 LS_deep%=\n\t"
        "s_waitcnt vmcnt(%[lowkeep])\n\t"
        "ds_write_b32 %[afull], %[vf]\n\t"
        "s_min_i32 %[t0], %[off], %[down]\n\t"
        "s_sub_u32 %[off], %[off], %[t0]\n\t"
        "s_sub_u32 %[t1], %[t1], %[t0]\n"
        "LS_next%=:\n\t"
        "s_add_u32 %[pair], %[pair], 1\n\t"
        "s_and_b32 %[pair], %[pair], 63\n\t"
        "v_readlane_b32 %[so], %[tab], %[pair]\n\t"
        "s_add_u32 %[n], %[n], 2\n\t"
        "s_cmp_lg_u32 %[n], %[end]\n\t"
        "s_cbranch_scc1 LS_top%=\n\t"
        "s_branch LS_done%=\n"
        "LS_waita%=:\n\t"
        "s_memrealtime vcc\n\t"
        "s_branch LS_spina%=\n"
        "LS_waitb%=:\n\t"
        "s_memrealtime vcc\n\t"
        "s_branch LS_spinb%=\n"
        "LS_deep%=:\n\t"
        "s_cmp_gt_u32 %[t0], %[encmid]\n\t"
        "s_cbranch_scc1 LS_deeper%=\n\t"
        "v_subrev_u32 %[vt], %[middiff], %[vf]\n\t"
        "s_waitcnt vmcnt(%[midkeep])\n\t"
        "s_branch LS_slow%=\n"
        "LS_deeper%=:\n\t"
        "v_subrev_u32 %[vt], %[capdiff], %[vf]\n\t"
        "s_waitcnt vmcnt(%[cap])\n"
        "LS_slow%=:\n\t"
        "ds_max_i32 %[afull], %[vt]\n\t"
        "s_cmp_gt_u32 %[t0], %[enchi]\n\t"
        "s_cselect_b32 %[t0], %[up], 0\n\t"
        "s_add_u32 %[off], %[off], %[t0]\n\t"
        "s_add_u32 %[t1], %[t1], %[t0]\n\t"
        "s_branch LS_next%=\n"
        "LS_space%=:\n\t"
        "s_mov_b32 m0, %[spincap]\n"
        "LS_again%=:\n\t"
        "ds_read_b32 %[vt], %[adone]\n\t"
        "s_waitcnt lgkmcnt(0)\n\t"
        "v_min_u32_dpp %[vt], %[vt], %[vt] quad_perm:[1,0,3,2] row_mask:0xf bank_mask:0xf\n\t"
        "s_nop 1\n\t"
        "v_min_u32_dpp %[vt], %[vt], %[vt] quad_perm:[2,3,0,1] row_mask:0xf bank_mask:0xf\n\t"
        "s_nop 1\n\t"
        "v_readfirstlane_b32 %[freeto], %[vt]\n\t"
        "s_nop 1\n\t"
        "s_add_u32 %[freeto], %[freeto], %[ns1]\n\t"
        "s_cmp_lt_i32 %[n], %[freeto]\n\t"
        "s_cbranch_scc1 LS_free%=\n\t"
        "s_sleep 1\n\t"
        "s_sub_u32 m0, m0, 1\n\t"
        "s_cmp_lg_u32 m0, 0\n\t"
        "s_cbranch_scc1 LS_again%=\n\t"
        "s_branch LS_free%=\n"
        "LS_done%=:\n\t"
        "s_waitcnt lgkmcnt(0)\n\t"
        "s_sub_u32 %[due], %[t1], %[off]\n\t"
        "s_mov_b32 %[now], vcc_lo"
        : [n] "+s"(n), [freeto] "+s"(freeto), [due] "+s"(due), [off] "+s"(off), [now] "+s"(now), [pos] "+s"(pos), [pair] "+s"(pair),
          [so] "=&s"(so), [vf] "+v"(vf), [t0] "=&s"(t0), [t1] "=&s"(t1), [vt] "=&v"(vt)
        : [srd] "s"(srd), [a0] "v"(va[0]), [a1] "v"(va[1]), [a2] "v"(va[2]), [a3] "v"(va[3]),
          [b0] "v"(vb[0]), [b1] "v"(vb[1]), [b2] "v"(vb[2]), [b3] "v"(vb[3]), [end] "s"(end),
          [afull] "v"(L_FULL), [adone] "v"(adone), [up] "s"(UP), [down] "s"(DOWN), [dt] "s"(dt), [enclow] "s"(ENC_LOW), [enchi] "s"(ENC_HI),
          [tab] "v"(tab), [ringend] "i"(NS * 4096), [lowkeep] "i"(LOWKEEP), [cap] "i"(CAP),
          [capdiff] "i"((CAP - LOWKEEP) / 4), [encmid] "i"(ENC_MID), [midkeep] "i"(MIDKEEP), [middiff] "i"((MIDKEEP - LOWKEEP) / 4),
          [ns1] "s"(room), [spincap] "i"(SPIN_CAP)
        : "memory", "scc", "vcc");
  }

  __device__ __forceinline__ void finish(v4u ro, u64 down, const int (&voff)[5], int groups) {
    int t0, t1, k, so = 0, vt = SLOT_W - 4;
#define FIN_PUB(keep) "s_waitcnt vmcnt(" #keep ")\n\tds_write_b32 %[afull], %[vt]\n\tv_add_u32 %[vt], 1, %[vt]\n\t"
#define FIN_LOAD(i) "buffer_load_dwordx4 %[v" #i "], %[r], %[so] offen nt lds\n\ts_add_u32 m0, m0, 0x400\n\t"
    asm volatile(
        FIN_PUB(16)
        "s_add_u32 %[t1], %[due], %[off]\n\t"
        "s_mov_b32 m0, %[wpos]\n\t"
        "s_mov_b32 %[k], 0\n"
        "FN_top%=:\n\t"
        "s_add_u32 %[t1], %[t1], %[wdt]\n"
        "FN_spin%=:\n\t"
        "s_memrealtime vcc\n\t"
        "s_waitcnt lgkmcnt(0)\n\t"
        "s_lshl_b32 %[t0], vcc_lo, 8\n\t"
        "s_sub_u32 %[t0], %[t0], %[t1]\n\t"
        "s_cmp_lt_i32 %[t0], 0\n\t"
        "s_cbranch_scc1 FN_spin%=\n\t"
        FIN_LOAD(0) "s_add_u32 %[k], %[k], 1\n\t" FIN_LOAD(1) "s_nop 0\n\t" FIN_LOAD(2) "s_nop 0\n\t" FIN_LOAD(3) "s_nop 0\n\t" FIN_LOAD(4)
        "s_add_u32 %[so], %[so], %[wstep]\n\t"
        "s_cmp_gt_u32 %[k], 3\n\t"
        "s_cbranch_scc1 FN_late%=\n\t"
        "s_cmp_eq_u32 %[k], 2\n\t"
        "s_cbranch_scc1 FN_two%=\n\t"
        "s_cmp_eq_u32 %[k], 3\n\t"
        "s_cbranch_scc1 FN_three%=\n\t"
        FIN_PUB(17)
        "s_branch FN_top%=\n"
        "FN_two%=:\n\t"
        FIN_PUB(18) FIN_PUB(14)
        "s_branch FN_top%=\n"
        "FN_three%=:\n\t"
        FIN_PUB(15)
        "s_branch FN_top%=\n"
        "FN_late%=:\n\t"
        "s_cmp_eq_u32 %[k], %[groups]\n\t"
        "s_cbranch_scc1 FN_done%=\n\t"
        "s_cmp_lg_u32 %[k], 4\n\t"
        "s_cbranch_scc1 FN_top%=\n\t"
        "s_mov_b64 s[40:41], %[rd01]\n\t"
        "s_mov_b32 %[so], 0\n\t"
        "s_branch FN_top%=\n"
        "FN_done%=:\n\t"
        "v_mov_b32 %[vt], %[end]\n\t"
        "s_waitcnt vmcnt(0)\n\t"
        "ds_write_b32 %[afull], %[vt]"
        : [t0] "=&s"(t0), [t1] "=&s"(t1), [k] "=&s"(k), [so] "+s"(so), [vt] "+v"(vt), [r] "+{s[40:43]}"(ro)
        : [v0] "v"(voff[0]), [v1] "v"(voff[1]), [v2] "v"(voff[2]), [v3] "v"(voff[3]), [v4] "v"(voff[4]),
          [rd01] "s"(down), [due] "s"(due), [off] "s"(off), [groups] "s"(groups), [afull] "v"(L_FULL),
          [wpos] "i"((SLOT_W % NS) * 4096), [wdt] "i"(DT + DT / 4), [wstep] "i"(32 * D * 2), [end] "i"(SLOT_END)
        : "memory", "scc", "vcc");
  }
};

__global__ void __launch_bounds__(384, 1)
fused_decode(const u16* __restrict__ x, const u16* __restrict__ wq, unsigned char* ws, const u16* __restrict__ Kc,
             const u16* __restrict__ Vc, const u16* __restrict__ wg, const u16* __restrict__ wu, const u16* __restrict__ wo,
             const u16* __restrict__ wd, u16* __restrict__ out) {
  const int lane = threadIdx.x & 63, w = __builtin_amdgcn_readfirstlane(threadIdx.x >> 6);
  const int g = lane >> 4, c = lane & 15;
  const int b = blockIdx.x, h = (b & 7) * 4 + (b >> 6), s = (b >> 3) & 7;
  const bool ffn = h < NFFN;
  const rsrc_t rws = mk_rsrc(ws, WS_END);

  asm volatile("" :: "s"(x), "s"(wq), "s"(ws), "s"(Kc), "s"(Vc), "s"(wg), "s"(wu), "s"(wo), "s"(wd), "s"(out));

  if (w == 4) {
    __builtin_amdgcn_s_setprio(3);
    __builtin_amdgcn_s_waitcnt(0);
    Loader ld;
    ld.n = NG; ld.freeto = NS - 1; ld.off = 0;
    if (lane < N_CTL) lds_st<unsigned>(L_DONE + lane * 4, 0u);
    asm volatile("s_waitcnt lgkmcnt(0)" ::: "memory");
    __builtin_amdgcn_s_barrier();
    int ln = lane;
    asm volatile("" : "+v"(ln));
    int vl[4], vm[4];
#pragma unroll
    for (int i = 0; i < 4; ++i) {
      vl[i] = ln * 16 + ((s & 1) << PS0) + (((s >> 1) & 1) << PS1) + ((s >> 2) << PS2) + ((i & 1) << PI0) + ((i >> 1) << PI1) - i * 1024;
      vm[i] = vl[i] + (1 << PT);
    }
    const size_t kvoff = (size_t)h * SK * DH;
    int tab = 0;
#pragma unroll
    for (int j = 0; j < 6; ++j) tab |= ((ln >> j) & 1) << PJ[j];
    int pos = NG * 4096;
    const unsigned gbytes = ffn ? D * F * 2 : 0;
    ld.start(mk_srd(wg, gbytes), mk_srd(wu, gbytes), (80 * s + (ln >> 3)) * (F * 2) + h * 128 + (ln & 7) * 16);
    int pair = 2 * k_rotation(ld.due - 4 * DT);
    int dt = DT;
#pragma nounroll
    for (int seg = 0; seg < 3; ++seg) {
      const v4u rs = mk_srd((seg < 2 ? Kc : Vc) + kvoff, SK * DH * 2);
      const int keep = seg == 2 ? 9 : 0;
      if (seg == 2) pair = 0;
      ld.freeto -= keep;
      ld.stream(rs, vl, vm, tab, pos, pair, seg == 0 ? NG + 64 : NG + 128 * seg, dt, NS - 1 - keep, L_DONE + (lane & 3) * 4);
      if (dt == DT) {
        const int lead = (int)ctl_read(L_LEAD);
        dt = DT + (seg == 0 ? lead / 192 : lead / 128);
      }
    }
    int l2 = lane;
    asm volatile("" : "+v"(l2));
    int vw[5];
#pragma unroll
    for (int i = 0; i < 5; ++i) { const int f = i * 64 + l2; vw[i] = ((f * 6554) >> 16) * (D * 2 - 160) + f * 16 + s * 160; }
    v4u ro = mk_srd(wo + (size_t)h * DH * D, DH * D * 2);
    u64 rd = (u64)(wd + (size_t)h * 64 * D);
    ld.finish(ro, rd, vw, ffn ? 6 : 4);
    return;
  }

  asm volatile("" ::: "memory");
  if (w == 5) {
    const unsigned tag = read_tag(ws);
    __builtin_amdgcn_s_barrier();
    const int col = 80 * s + 8 * (h % 10) + 4 * (lane & 1);
    const unsigned xr = x[col + g];
    const int vy = WS_XY + (s * 32 + (lane >> 1)) * 640 + (h % 10) * 64 + (lane & 1) * 32;
    {
      ctl_wait_ge<8>(L_FULL, T_PUT);
      const unsigned org = lds_ld<unsigned>(L_ORG);
      if (b < 8) st_pair<18>(rws, WS_XT + b * 1024 + lane * 16, __uint_as_float(org), 0.f, tag);
      ctl_wait_ge<8>(L_FULL, T_GET);
      v4u t;
      const bool all = wait_pair(ws, WS_XT + (lane & 7) * 1024 + (b & 7) * 128, tag, t, 4);
      const float lead = sum8((float)(int)(t.x - org)) * 0.125f;
      if (all) lds_st<int>(L_LEAD, (int)fminf(fmaxf(lead, -(float)(SKEWMAX << 8)), (float)(SKEWMAX << 8)));
    }
    int redb = L_RED + lane * 8;
    asm volatile("" : "+v"(redb));
    const int o = WS_XG + (h * 8 + s) * XG_STRIDE, po = WS_XG + (h * 8 + (lane & 7)) * XG_STRIDE;
    if (ffn) {
      ctl_wait_ge<8>(C_G, 4);
      ctl_wait_ge<8>(L_FULL, G_PUT);
      const v2f p = red_sum<2048>(redb);
      st_pair<18>(rws, o + lane * 16, p.x, p.y, tag);
      ctl_wait_ge<8>(L_FULL, G_GET);
      v4u a[8];
      wait_pairs8(ws, po + (lane >> 3) * 16, tag, a);
      float gv[8], uv[8];
#pragma unroll
      for (int k = 0; k < 4; ++k) {
        gv[2 * k] = __uint_as_float(a[k].x); gv[2 * k + 1] = __uint_as_float(a[k].z);
        uv[2 * k] = __uint_as_float(a[k + 4].x); uv[2 * k + 1] = __uint_as_float(a[k + 4].z);
      }
#pragma unroll
      for (int i = 0; i < 4; ++i) {
        const float gl = gv[i] + dpp<HMIR>(gv[i]), gh = gv[i + 4] + dpp<HMIR>(gv[i + 4]), ul = uv[i] + dpp<HMIR>(uv[i]), uh = uv[i + 4] + dpp<HMIR>(uv[i + 4]);
        gv[i] = (lane & 4) ? gh : gl; uv[i] = (lane & 4) ? uh : ul;
      }
#pragma unroll
      for (int i = 0; i < 2; ++i) {
        const float gl = gv[i] + dpp<XOR2>(gv[i]), gh = gv[i + 2] + dpp<XOR2>(gv[i + 2]), ul = uv[i] + dpp<XOR2>(uv[i]), uh = uv[i + 2] + dpp<XOR2>(uv[i + 2]);
        gv[i] = (lane & 2) ? gh : gl; uv[i] = (lane & 2) ? uh : ul;
      }
      const float g0 = gv[0] + dpp<XOR1>(gv[0]), g1 = gv[1] + dpp<XOR1>(gv[1]), u0 = uv[0] + dpp<XOR1>(uv[0]), u1 = uv[1] + dpp<XOR1>(uv[1]);
      const float gt = rbf((lane & 1) ? g1 : g0), up = rbf((lane & 1) ? u1 : u0);
      const int e = 32 * ((lane >> 2) & 1) + 16 * ((lane >> 3) & 1) + 8 * (lane & 1) + 4 * ((lane >> 1) & 1) + (lane >> 4);
      lds_st<unsigned>(L_H + e * 4, f2bf(rbf(gt * __builtin_amdgcn_rcpf(1.f + __expf(-gt))) * up));
    }
    ctl_wait_ge<8>(C_L, 4);
    {
      const v4f lw = lds_ld<v4f>(L_L), mw = lds_ld<v4f>(L_M);
      const float M = fmaxf(fmaxf(mw.x, mw.y), fmaxf(mw.z, mw.w));
      if (lane == 0) st_pair<18>(rws, o + 1024, M, (lw.x + lw.y) + (lw.z + lw.w), tag);
      ctl_wait_ge<8>(L_FULL, SLOT_V + M_GET);
      v4u ml;
      wait_pair(ws, po + 1024, tag, ml, SPIN_CAP);
      const float mp = __uint_as_float(ml.x);
      float MH = fmaxf(mp, dpp<XOR1>(mp)); MH = fmaxf(MH, dpp<XOR2>(MH)); MH = fmaxf(MH, dpp<HMIR>(MH));
      float L = __builtin_amdgcn_exp2f(mp - MH) * __uint_as_float(ml.z);
      L += dpp<XOR1>(L); L += dpp<XOR2>(L); L += dpp<HMIR>(L);
      lds_st<float>(L_SCALE, __builtin_amdgcn_exp2f(M - MH) / L);
      ctl_write(C_GRDY, 1u);
    }
    if (h >= 10) return;
    ctl_wait_ge<1>(C_YGO, 1);
    __builtin_amdgcn_s_sleep(YWAIT);
    v4u a, bb;
    look2(ws, vy, a, bb);
    finish2(ws, vy, tag, a, bb);
    auto s32 = __builtin_amdgcn_permlane32_swap(a.x, bb.x, false, false), t32 = __builtin_amdgcn_permlane32_swap(a.z, bb.z, false, false);
    const float p0 = __uint_as_float(s32[0]) + __uint_as_float(s32[1]), p1 = __uint_as_float(t32[0]) + __uint_as_float(t32[1]);
    auto s16 = __builtin_amdgcn_permlane16_swap(__float_as_uint(p0), __float_as_uint(p1), false, false);
    float y = __uint_as_float(s16[0]) + __uint_as_float(s16[1]);
    y += dpp<XOR2>(y); y += dpp<ROR4>(y); y += dpp<ROR8>(y);
    if (c < 2) __hip_atomic_store(out + col + g, (u16)f2bf(bf2f(xr) + y), __ATOMIC_RELAXED, __HIP_MEMORY_SCOPE_AGENT);
    if (b == 0 && lane == 0) __hip_atomic_store((unsigned*)(ws + WS_EPOCH), tag, __ATOMIC_RELAXED, __HIP_MEMORY_SCOPE_AGENT);
    return;
  }

  v4u wqf[5];
  unsigned xv[5], xq[5], xg[5];
#pragma unroll
  for (int i = 0; i < 5; ++i) xv[i] = ((const unsigned*)x)[lane + 64 * i];
#pragma unroll
  for (int t = 0; t < 5; ++t) xq[t] = x[80 * s + 20 * w + 4 * t + g];
  __builtin_amdgcn_sched_barrier(0);
#pragma unroll
  for (int t = 0; t < 5; ++t)
    wqf[t] = __builtin_amdgcn_raw_buffer_load_b128(mk_rsrc(wq, D * H * DH * 2), g * (H * DH * 2) + c * 16, (80 * s + 20 * w + 4 * t) * (H * DH * 2) + h * (DH * 2), 2);
  __builtin_amdgcn_sched_barrier(0);
#pragma unroll
  for (int t = 0; t < 5; ++t) xg[t] = x[80 * s + 16 * t + 8 * (w >> 1) + 4 * (w & 1) + g];
  __builtin_amdgcn_sched_barrier(0);
  unsigned epoch;
  asm volatile("s_load_dword %0, %1, 0x0" : "=s"(epoch) : "s"(ws + WS_EPOCH));
  __builtin_amdgcn_s_barrier();
  asm volatile("ds_write_b32 %0, %1" :: "v"(C_KGO), "v"(1u), "v"(xv[0]) : "memory");
  __builtin_amdgcn_sched_barrier(0);
  Diag dg;
  dg.init(c);
  float sum = 0.f;
#pragma unroll
  for (int i = 0; i < 5; ++i) sum += bf2f(xv[i] & 0xffffu) + bf2f(xv[i] >> 16);
  const float mean = wave_sum(sum) * (1.f / D);
  float var = 0.f;
#pragma unroll
  for (int i = 0; i < 5; ++i) { const float d0 = bf2f(xv[i] & 0xffffu) - mean, d1 = bf2f(xv[i] >> 16) - mean; var += d0 * d0 + d1 * d1; }
  var = wave_sum(var) * (1.f / D);
  const float mb = rbf(mean), rden = 1.f / rbf(sqrtf(rbf(rbf(var) + 1e-5f)));
#pragma unroll
  for (int t = 0; t < 5; ++t) xq[t] = f2bf(rbf(bf2f(xq[t]) - mb) * rden);

  int full = 0;
  int redb = L_RED + lane * 8;
  asm volatile("" : "+v"(redb));
  const int redpos = w * 512 + (g * 8 + c) * 16;
  {
    v4f aq = {0.f, 0.f, 0.f, 0.f};
#pragma unroll
    for (int t = 0; t < 5; ++t) aq = mfma(wqf[t], dg.frag(xq[t]), aq);
    if (c < 8) lds_st<v4f>(L_RED + redpos, aq);
  }
  const bool qlast = ctl_arrive(C_Q) == 3;
  asm volatile("s_waitcnt lgkmcnt(0)" : "+s"(epoch));
  const unsigned tag = epoch + 1u;
  if (qlast) {
    const v2f p = red_sum<0>(redb);
    st_pair(rws, WS_XQ + (h * 8 + s) * 1024 + lane * 16, p.x, p.y, tag);
    ctl_write(C_QGO, 1u);
  }
  const int gpos = (h * 8 + (c & 7)) * 1024 + (32 * w + 8 * g + 4 * (c >> 3)) * 8;
  {
    ctl_wait_ge<1>(C_QGO, 1);
    __builtin_amdgcn_s_sleep(QWAIT);
    v4u a, bq;
    asm volatile("" :: "v"(xg[0]), "v"(xg[1]), "v"(xg[2]), "v"(xg[3]), "v"(xg[4]));
    look2(ws, WS_XQ + gpos, a, bq);
    if (ffn) {
      v4f ag = {0.f, 0.f, 0.f, 0.f};
      wait_full(full, NG);
      const int ao = L_RING + (w >> 1) * 2048 + (c >> 3) * 1024 + (4 * (w & 1) + g) * 128 + (c & 7) * 16;
#pragma unroll
      for (int t = 0; t < 5; ++t) ag = mfma(lds_ld<v4u>(ao + t * 4096), dg.frag(f2bf(rbf(bf2f(xg[t]) - mb) * rden)), ag);
      if (c < 8) lds_st<v4f>(L_RED + 2048 + redpos, ag);
      ctl_arrive(C_G);
    }
    ctl_write(L_DONE + w * 4, NG + w);
    finish2(ws, WS_XQ + gpos, tag, a, bq);
    float v[4];
    sum8x4(a, bq, v);
#pragma unroll
    for (int k = 0; k < 4; ++k) lds_st<u16>(L_Q + (32 * w + 8 * k + 2 * g + (c >> 3)) * 2, (u16)f2bf(v[k]));
    ctl_arrive(C_QRDY);
    ctl_wait_ge<0>(C_QRDY, 4);
  }
  bf16x8 qf[4];
#pragma unroll
  for (int i = 0; i < 4; ++i) qf[i] = __builtin_bit_cast(bf16x8, lds_ld<v4u>(L_Q + (((4 * i + g) ^ c) * 16)));

  const int sc = L_SC + w * 2048;
  int pos = (NG + w) * 4096;
  wait_full(full, NG + w + 1);
  const int rot = k_rotation(ctl_read(L_ORG));
#pragma nounroll
  for (int it = 0; it < 32; ++it) {
    const int n = NG + 4 * it + w;
    wait_full(full, n + 1);
    const int base = L_RING + pos + c * 256;
    v4u kf[4];
#pragma unroll
    for (int i = 0; i < 4; ++i) kf[i] = lds_ld<v4u>(base + (((4 * i + g) ^ c) * 16));
    ctl_write(L_DONE + w * 4, n + 4);
    pos += 4 * 4096; pos = pos >= NS * 4096 ? pos - NS * 4096 : pos;
    v4f d0 = {0.f, 0.f, 0.f, 0.f}, d1 = d0;
    d0 = mfma(kf[0], qf[0], d0); d1 = mfma(kf[1], qf[1], d1);
    d0 = mfma(kf[2], qf[2], d0); d1 = mfma(kf[3], qf[3], d1);
    d0 += d1;
    const float dv = (c & 2) ? ((c & 1) ? d0.w : d0.z) : ((c & 1) ? d0.y : d0.x);
    const float sv = rbf(rbf(dv) * 0.088388347648318440f) * 1.4426950408889634f;
    if ((c >> 2) == g) lds_st<float>(sc + (((it + rot) & 31) * 16 + c) * 4, sv);
  }
  {
    v4f s0 = lds_ld<v4f>(sc + lane * 16), s1 = lds_ld<v4f>(sc + 1024 + lane * 16);
    const float mw = wave_max(fmaxf(fmaxf(fmaxf(s0.x, s0.y), fmaxf(s0.z, s0.w)), fmaxf(fmaxf(s1.x, s1.y), fmaxf(s1.z, s1.w))));
    lds_st<float>(L_M + w * 4, mw);
    ctl_arrive(C_M);
    ctl_wait_ge<0>(C_M, 4);
    const v4f m4 = lds_ld<v4f>(L_M);
    const float M = fmaxf(fmaxf(m4.x, m4.y), fmaxf(m4.z, m4.w));
    v4u p0, p1;
    float l = 0.f;
#pragma unroll
    for (int k = 0; k < 4; ++k) {
      const float a = __builtin_amdgcn_exp2f(s0[k] - M), bq = __builtin_amdgcn_exp2f(s1[k] - M);
      l += a + bq;
      const unsigned ah = f2bf(a), bh = f2bf(bq);
      p0[k] = ah | (f2bf(a - bf2f(ah)) << 16);
      p1[k] = bh | (f2bf(bq - bf2f(bh)) << 16);
    }
    lds_st<float>(L_L + w * 4, wave_sum(l));
    lds_st<v4u>(sc + lane * 16, p0); lds_st<v4u>(sc + 1024 + lane * 16, p1);
    ctl_arrive(C_L);
  }
  v4f o0 = {0.f, 0.f, 0.f, 0.f}, o1 = o0;
  float scale = 0.f;
  unsigned hv[4] = {0u, 0u, 0u, 0u};
  static_assert(SLOT_V % NS + 4 <= NS, "first V slots do not wrap");
  pos = (SLOT_V % NS + w) * 4096;
#pragma nounroll
  for (int it = 0; it < 32; ++it) {
    const int n = SLOT_V + 4 * it + w;
    const v4u p = lds_ld<v4u>(sc + (it * 16 + 4 * g) * 4);
    bf16x8 pf[4];
#pragma unroll
    for (int t = 0; t < 4; ++t) pf[t] = dg.frag(p[t]);
    if (it == 30) {
      ctl_wait_ge<1>(C_GRDY, 1);
      scale = lds_ld<float>(L_SCALE);
#pragma unroll
      for (int t = 0; t < 4; ++t) hv[t] = lds_ld<unsigned>(L_H + (16 * w + 4 * t + g) * 4);
    }
    wait_full(full, n + 1);
    const int base = L_RING + pos + g * 1024 + c * 16;
    v4u vf[4];
#pragma unroll
    for (int t = 0; t < 4; ++t) vf[t] = lds_ld<v4u>(base + t * 256);
    ctl_write(L_DONE + w * 4, n + 4);
    pos += 4 * 4096; pos = pos >= NS * 4096 ? pos - NS * 4096 : pos;
    o0 = mfma(vf[0], pf[0], o0); o1 = mfma(vf[1], pf[1], o1);
    o0 = mfma(vf[2], pf[2], o0); o1 = mfma(vf[3], pf[3], o1);
  }
  o0 += o1;
#pragma unroll
  for (int k = 0; k < 4; ++k) o0[k] += dpp<ROR8>(o0[k]);
  if (c < 8) lds_st<v4f>(L_RED + redpos, o0);
  if (ctl_arrive(C_O) == 3) {
    const v2f a = scale * red_sum<0>(redb);
    st_pair(rws, WS_XO + (h * 8 + s) * 1024 + lane * 16, a.x, a.y, tag);
    ctl_write(C_OGO, 1u);
  }
  {
    v4u a, bq;
    ctl_wait_ge<0>(C_OGO, 1);
    __builtin_amdgcn_s_sleep(OWAIT);
    look2(ws, WS_XO + gpos, a, bq);
    const int wbase = L_RING + (SLOT_W % NS) * 4096 + c * 16;
    wait_full(full, SLOT_END);
    v4u wf[8];
#pragma unroll
    for (int t = 0; t < 8; ++t) wf[t] = lds_ld<v4u>(wbase + (32 * w + 8 * (t >> 1) + 2 * g + (t & 1)) * 160);
    v4f y0 = {0.f, 0.f, 0.f, 0.f}, y1 = y0;
    if (ffn) {
#pragma unroll
      for (int t = 0; t < 4; ++t) y0 = mfma(lds_ld<v4u>(wbase + 5 * 4096 + (16 * w + 4 * t + g) * 160), dg.frag(hv[t]), y0);
    }
    asm volatile("" : "+v"(wf[0]), "+v"(wf[1]), "+v"(wf[2]), "+v"(wf[3]), "+v"(wf[4]), "+v"(wf[5]), "+v"(wf[6]), "+v"(wf[7]), "+v"(y0));
    finish2(ws, WS_XO + gpos, tag, a, bq);
    float v[4];
    sum8x4(a, bq, v);
    bf16x8 of[4];
#pragma unroll
    for (int k = 0; k < 4; ++k) of[k] = dg.frag(__builtin_bit_cast(unsigned, __builtin_convertvector(v2f{v[k], v[k]}, bf16x2)));
    __builtin_amdgcn_sched_barrier(0);
#pragma unroll
    for (int k = 0; k < 4; ++k) { y1 = mfma(wf[2 * k + 1], of[k], y1); y0 = mfma(wf[2 * k], of[k], y0); }
#pragma unroll
    for (int k = 0; k < 4; ++k) y0[k] += dpp<ROR8>(y1[k]);
    if (c < 8) {
#pragma unroll
      for (int k = 0; k < 4; ++k) lds_st<float>(L_RED + 4096 + w * 512 + (8 * (4 * g + k) + c) * 4, y0[k]);
    }
  }
  if (ctl_arrive(C_Y) == 3) {
    const v2f a = red_sum<4096>(redb);
    if (lane < 40) st_pair(rws, WS_XY + (s * 32 + h) * 640 + lane * 16, a.x, a.y, tag);
    ctl_write(C_YGO, 1u);
  }
}

}

extern "C" void kernel_launch(void* const* d_in, const int* in_sizes, int n_in, void* d_out, int out_size, void* d_ws,
                              size_t ws_size, hipStream_t stream) {
  (void)in_sizes; (void)n_in; (void)out_size; (void)ws_size;
  (void)hipFuncSetAttribute((const void*)fused_decode, hipFuncAttributeMaxDynamicSharedMemorySize, LDS_BYTES);
  fused_decode<<<256, 384, LDS_BYTES, stream>>>((const u16*)d_in[0], (const u16*)d_in[3], (unsigned char*)d_ws, (const u16*)d_in[1],
                                               (const u16*)d_in[2], (const u16*)d_in[6], (const u16*)d_in[7], (const u16*)d_in[4],
                                               (const u16*)d_in[8], (u16*)d_out);
}
```
